```python
import torch

def _softmax(x, axis=-1):
    return torch.softmax(x, dim=axis)
def _gelu(x):
    return torch.nn.functional.gelu(x, approximate="tanh")
def _silu(x):
    return torch.nn.functional.silu(x)
def _sigmoid(x):
    return torch.sigmoid(x)
def _layernorm(x, axis=-1, eps=1e-5):
    m = x.mean(dim=axis, keepdim=True)
    v = x.var(dim=axis, keepdim=True, unbiased=False)
    return (x - m) / torch.sqrt(v + eps)
def _scatter_add(tgt, idx, src, axis=0):
    return tgt.clone().index_add_(axis, idx, src)
def _segment_sum(src, seg, n):
    out = torch.zeros((n,) + src.shape[1:], dtype=src.dtype)
    return out.index_add_(0, seg, src)
def _pad(x, pw):
    flat = tuple(v for pair in reversed(pw) for v in pair)
    return torch.nn.functional.pad(x, flat)

kernel_name = 'standalone__channel_attn_se__vision'

def setup_inputs():

    _w = (lambda a: (lambda t: t.to(torch.bfloat16) if t.is_floating_point() else t)(torch.as_tensor(a)))
    H = W = 512
    C = 192
    r = max(C // 16, 4)
    return {
        "x": _w((lambda *s: torch.randn(*s, dtype=torch.bfloat16) * 3.2)(1, H, W, C)),
        "w_down": _w((lambda *s: torch.randn(*s, dtype=torch.bfloat16) * 3.2)(C, r)),
        "w_up": _w((lambda *s: torch.randn(*s, dtype=torch.bfloat16) * 3.2)(r, C)),
    }

def reference(x, w_down, w_up):
    s = x.mean(axis=(1, 2))
    h = _silu(torch.einsum('bc,cr->br', s, w_down))
    g = _sigmoid(torch.einsum('br,rc->bc', h, w_up))
    return x * g[:, None, None, :]
```

```cpp
#include <hip/hip_runtime.h>
#include <hip/hip_bf16.h>
#include <stdint.h>

#define LOAD_AUX 2
#define STORE_AUX 0

typedef unsigned int u32x4 __attribute__((ext_vector_type(4)));
typedef unsigned int u32x2 __attribute__((ext_vector_type(2)));
typedef float f32x2 __attribute__((ext_vector_type(2)));
typedef float f32x4 __attribute__((ext_vector_type(4)));
typedef __bf16 bf16x2 __attribute__((ext_vector_type(2)));
typedef unsigned long long u64;
#define GAS __attribute__((address_space(1)))
#define CAS __attribute__((address_space(4)))
#define RLX_AGENT __ATOMIC_RELAXED, __HIP_MEMORY_SCOPE_AGENT

constexpr int C = 192;
constexpr int R = 12;
constexpr int NPIX = 512 * 512;
constexpr int NWG = 256;
constexpr int NT = 768;
constexpr int NI = 32;
constexpr unsigned CHUNK_BYTES = NT * NI * 16;
constexpr unsigned TOTAL_BYTES = CHUNK_BYTES * NWG;
constexpr int NRED = 24;
constexpr unsigned SPIN_CAP = 1u << 17;

constexpr unsigned WS_EPOCH = 0;
constexpr unsigned WS_S = 1024;
constexpr unsigned WS_P = 4096;

__device__ __forceinline__ float bf_lo(unsigned u) { return __uint_as_float(u << 16); }
__device__ __forceinline__ float bf_hi(unsigned u) { return __uint_as_float(u & 0xffff0000u); }
__device__ __forceinline__ unsigned pack_bf16(float a, float b) {
  f32x2 v = {a, b};
  bf16x2 r = __builtin_convertvector(v, bf16x2);
  return __builtin_bit_cast(unsigned, r);
}
__device__ __forceinline__ float round_bf16(float a) { return bf_lo(pack_bf16(a, 0.f)); }

__global__ __launch_bounds__(NT, 3) void se_fused(const unsigned short* __restrict__ x,
                                                  const unsigned short* __restrict__ wd,
                                                  const unsigned short* __restrict__ wu,
                                                  unsigned short* __restrict__ out, unsigned char* ws) {
  __shared__ __attribute__((aligned(16))) float sm[NT * 8];
  __shared__ __attribute__((aligned(16))) float s_s[C];
  __shared__ __attribute__((aligned(16))) float s_h[16];
  __shared__ __attribute__((aligned(16))) float s_g[C];
  const int t = threadIdx.x;
  const int lane = t & 63;
  const int wave = __builtin_amdgcn_readfirstlane(t >> 6);
  const int b = blockIdx.x;

  const __amdgpu_buffer_rsrc_t rx =
      __builtin_amdgcn_make_buffer_rsrc((void*)x, 0, TOTAL_BYTES, 0x00020000);
  const __amdgpu_buffer_rsrc_t ro =
      __builtin_amdgcn_make_buffer_rsrc((void*)out, 0, TOTAL_BYTES, 0x00020000);
  const unsigned voff = (unsigned)t * 16u;
  const unsigned sbase = (unsigned)b * CHUNK_BYTES;

  u32x4 d[NI];
#pragma unroll
  for (int i = 0; i < NI; i++)
    d[i] = __builtin_amdgcn_raw_buffer_load_b128(rx, voff, sbase + (unsigned)i * (NT * 16u), LOAD_AUX);

  __builtin_amdgcn_sched_barrier(0);
  const unsigned tag = *(const CAS unsigned*)(ws + WS_EPOCH) + 1u;

  f32x2 acc[4] = {{0.f, 0.f}, {0.f, 0.f}, {0.f, 0.f}, {0.f, 0.f}};
#pragma unroll
  for (int i = 0; i < NI; i++) {
#pragma unroll
    for (int j = 0; j < 4; j++) acc[j] += (f32x2){bf_lo(d[i][j]), bf_hi(d[i][j])};
    asm volatile("" : "+v"(d[i]), "+v"(acc[0]), "+v"(acc[1]), "+v"(acc[2]), "+v"(acc[3]));
  }
  *(f32x4*)&sm[t * 8] = (f32x4){acc[0][0], acc[0][1], acc[1][0], acc[1][1]};
  *(f32x4*)&sm[t * 8 + 4] = (f32x4){acc[2][0], acc[2][1], acc[3][0], acc[3][1]};
  __syncthreads();
  GAS u64* P = (GAS u64*)(ws + WS_P);
  GAS u64* S = (GAS u64*)(ws + WS_S);
  if (t < C) {
    float s0 = 0.f, s1 = 0.f;
#pragma unroll
    for (int k = 0; k < NT / 24; k += 2) { s0 += sm[k * C + t]; s1 += sm[(k + 1) * C + t]; }
    const float s = s0 + s1;
    __hip_atomic_store(P + ((size_t)(t >> 3) * NWG + b) * 8 + (t & 7), ((u64)tag << 32) | __float_as_uint(s), RLX_AGENT);
  }
  float wdv[3];
#pragma unroll
  for (int k = 0; k < 3; k++) wdv[k] = bf_lo(wd[(lane + 64 * k) * R + wave]);
  float wuv[R];
  const int cc = t < C ? t : 0;
#pragma unroll
  for (int k = 0; k < R; k++) wuv[k] = bf_lo(wu[k * C + cc]);

  if (b < NRED) {
    __syncthreads();
    if (wave < 8) {
      GAS u64* src = P + (size_t)b * NWG * 8 + wave * 256 + lane;
      float v[4];
      for (unsigned spins = 0;; spins++) {
        bool ok = true;
#pragma unroll
        for (int k = 0; k < 4; k++) {
          const u64 g = __hip_atomic_load(src + k * 64, RLX_AGENT);
          v[k] = __uint_as_float((unsigned)g);
          ok &= (unsigned)(g >> 32) == tag;
        }
        if (__all(ok) || spins > SPIN_CAP) break;
        __builtin_amdgcn_s_sleep(1);
      }
      float a = (v[0] + v[1]) + (v[2] + v[3]);
      a += __shfl_xor(a, 8);
      a += __shfl_xor(a, 16);
      a += __shfl_xor(a, 32);
      if (lane < 8) sm[wave * 8 + lane] = a;
    }
    __syncthreads();
    if (t < 8) {
      float a = 0.f;
#pragma unroll
      for (int k = 0; k < 8; k++) a += sm[k * 8 + t];
      a = round_bf16(a * (1.0f / NPIX));
      __hip_atomic_store(S + b * 8 + t, ((u64)tag << 32) | __float_as_uint(a), RLX_AGENT);
    }
  }
  if (wave == 0) {
    float v[3];
    for (unsigned spins = 0;; spins++) {
      bool ok = true;
#pragma unroll
      for (int k = 0; k < 3; k++) {
        const u64 g = __hip_atomic_load(S + k * 64 + lane, RLX_AGENT);
        v[k] = __uint_as_float((unsigned)g);
        ok &= (unsigned)(g >> 32) == tag;
      }
      if (__all(ok) || spins > SPIN_CAP) break;
      __builtin_amdgcn_s_sleep(1);
    }
#pragma unroll
    for (int k = 0; k < 3; k++) s_s[k * 64 + lane] = v[k];
    if (b == 0 && lane == 0) __hip_atomic_store((GAS unsigned*)(ws + WS_EPOCH), tag, RLX_AGENT);
  }
  __syncthreads();
  {
    float a = s_s[lane] * wdv[0] + s_s[lane + 64] * wdv[1] + s_s[lane + 128] * wdv[2];
#pragma unroll
    for (int m = 32; m >= 1; m >>= 1) a += __shfl_xor(a, m);
    a = round_bf16(a);
    a = round_bf16(a / (1.0f + __expf(-a)));
    if (lane == 0) s_h[wave] = a;
  }
  __syncthreads();
  if (t < C) {
    float a = 0.f;
#pragma unroll
    for (int k = 0; k < R; k++) a += s_h[k] * wuv[k];
    a = round_bf16(a);
    s_g[t] = round_bf16(1.0f / (1.0f + __expf(-a)));
  }
  __syncthreads();
  const f32x4 g0 = *(const f32x4*)&s_g[(t % 24) * 8];
  const f32x4 g1 = *(const f32x4*)&s_g[(t % 24) * 8 + 4];
#pragma unroll
  for (int i = 0; i < NI; i++) {
    u32x4 o;
    o[0] = pack_bf16(bf_lo(d[i][0]) * g0[0], bf_hi(d[i][0]) * g0[1]);
    o[1] = pack_bf16(bf_lo(d[i][1]) * g0[2], bf_hi(d[i][1]) * g0[3]);
    o[2] = pack_bf16(bf_lo(d[i][2]) * g1[0], bf_hi(d[i][2]) * g1[1]);
    o[3] = pack_bf16(bf_lo(d[i][3]) * g1[2], bf_hi(d[i][3]) * g1[3]);
    __builtin_amdgcn_raw_buffer_store_b128(o, ro, voff, sbase + (unsigned)i * (NT * 16u), STORE_AUX);
  }
}

extern "C" void kernel_launch(void* const* d_in, const int* in_sizes, int n_in, void* d_out, int out_size,
                              void* d_ws, size_t ws_size, hipStream_t stream) {
  se_fused<<<NWG, NT, 0, stream>>>((const unsigned short*)d_in[0], (const unsigned short*)d_in[1],
                                   (const unsigned short*)d_in[2], (unsigned short*)d_out, (unsigned char*)d_ws);
}
```

```cpp
#include <hip/hip_runtime.h>
#include <stdint.h>

typedef unsigned long long u64;
#define LAS __attribute__((address_space(3)))

__global__ __launch_bounds__(768, 3) void se_fused(const void* x, const void* w_down, const void* w_up, void* out,
                                                   void* ws) {
  __shared__ __attribute__((aligned(16))) char smem[38016];
  asm volatile(
      "\ts_load_dwordx2 s[64:65], %[ka], 0x0\n"
      "\tv_lshlrev_b32 v15, 4, v0\n"
      "\ts_mul_i32 s68, %[b], 0x3000\n"
      "\ts_lshl_b32 s66, 3, 25\n"
      "\ts_lshl_b32 s67, 1, 17\n"
      "\ts_waitcnt lgkmcnt(0)\n"
      "\ts_pack_ll_b32_b16 s65, s65, 0\n"
      "\tbuffer_load_dwordx4 v[40:43], v15, s[64:67], s68 offen nt\n"
      "\ts_memrealtime s[72:73]\n"
      "\tv_lshrrev_b32 v1, 6, v0\n"
      "\ts_load_dwordx4 s[8:11], %[ka], 0x8\n"
      "\ts_load_dwordx4 s[12:15], %[ka], 0x18\n"
      "\tv_readfirstlane_b32 s24, v1\n"
      "\ts_lshl_b32 s69, 3, 20\n"
      "\ts_mov_b32 s74, 0\n"
      "\ts_mov_b32 s94, 0\n"
      "\ts_getpc_b64 s[22:23]\n"
      ".Lse_pc:\n"
      "\ts_movk_i32 s79, 660\n"
      "\ts_mov_b32 s84, s68\n"
      "\ts_add_u32 s22, s22, .Lse_tab-.Lse_pc-8\n"
      "\ts_addc_u32 s23, s23, 0\n"
      "\ts_mul_i32 s70, %[b], 12\n"
      "\ts_add_u32 s70, s70, s24\n"
      "\ts_mul_i32 s70, s70, 14080\n"
      "\ts_lshl_b32 s75, -1, 16\n"
      "\ts_lshr_b32 s70, s70, 16\n"
      "\ts_addk_i32 s70, -250\n"
      "\tv_mov_b64 v[2:3], 0\n"
      "\tv_mov_b64 v[4:5], 0\n"
      "\tv_mov_b64 v[6:7], 0\n"
      "\tv_mov_b64 v[8:9], 0\n"
      "\ts_lshr_b32 s30, %[b], 4\n"
      "\ts_xor_b32 s34, s24, 3\n"
      "\ts_or_b32 s30, s30, s34\n"
      "\ts_cbranch_scc1 .Lse_cp\n"
      "\ts_call_b64 s[92:93], .Lse_cpf\n"
      ".Lse_cp:\n"
      "\ts_waitcnt lgkmcnt(0)\n"
      "\ts_mov_b32 s71, s72\n"
      "\ts_call_b64 s[76:77], .Lse_sub\n"
      "\ts_branch .Lse_main\n"
      ".Lse_sub:\n"
      "\ts_add_u32 s68, s68, s69\n"
      "\ts_add_i32 s70, s70, s79\n"
      "\ts_add_u32 s94, s94, 1\n"
      "\ts_mov_b32 s78, 0\n"
      ".Lse_a:\n"
      "\ts_memrealtime s[72:73]\n"
      "\ts_getreg_b32 s30, hwreg(HW_REG_IB_STS)\n"
      "\ts_bfe_u32 s31, s30, 0x20016\n"
      "\ts_and_b32 s30, s30, 15\n"
      "\ts_lshl4_add_u32 s30, s31, s30\n"
      "\ts_cmp_lt_u32 s30, s94\n"
      "\ts_cbranch_scc0 .Lse_t\n"
      ".Lse_accj:\n"
      "\ts_lshl_b32 s31, s94, 3\n"
      "\ts_add_u32 s90, s22, s31\n"
      "\ts_addc_u32 s91, s23, 0\n"
      "\ts_setpc_b64 s[90:91]\n"
      ".Lse_acc:\n"
      "\ts_set_gpr_idx_on s74, gpr_idx(SRC1)\n"
      "\tv_lshlrev_b32 v24, 16, v40\n"
      "\tv_and_b32 v25, s75, v40\n"
      "\tv_lshlrev_b32 v26, 16, v41\n"
      "\tv_and_b32 v27, s75, v41\n"
      "\tv_lshlrev_b32 v28, 16, v42\n"
      "\tv_and_b32 v29, s75, v42\n"
      "\tv_lshlrev_b32 v30, 16, v43\n"
      "\tv_and_b32 v31, s75, v43\n"
      "\ts_set_gpr_idx_off\n"
      "\ts_sub_u32 s94, s94, 1\n"
      "\tv_pk_add_f32 v[2:3], v[2:3], v[24:25]\n"
      "\tv_pk_add_f32 v[4:5], v[4:5], v[26:27]\n"
      "\tv_pk_add_f32 v[6:7], v[6:7], v[28:29]\n"
      "\tv_pk_add_f32 v[8:9], v[8:9], v[30:31]\n"
      "\ts_add_i32 s74, s74, 4\n"
      "\ts_cmp_eq_u32 s79, 0\n"
      "\ts_cbranch_scc1 .Lse_subret\n"
      ".Lse_t:\n"
      "\ts_add_u32 s78, s78, 1\n"
      "\ts_waitcnt lgkmcnt(0)\n"
      "\ts_sub_u32 s72, s72, s71\n"
      "\ts_lshl_b32 s72, s72, 4\n"
      "\ts_cmp_ge_i32 s72, s70\n"
      "\ts_cbranch_scc1 .Lse_subret\n"
      "\ts_sleep 1\n"
      "\ts_cmp_lt_u32 s78, 0x1000\n"
      "\ts_cbranch_scc1 .Lse_a\n"
      ".Lse_subret:\n"
      "\ts_setpc_b64 s[76:77]\n"
      ".Lse_tab:\n"
      "\ts_waitcnt vmcnt(0)\n"
      "\ts_branch .Lse_acc\n"
      "\ts_waitcnt vmcnt(1)\n"
      "\ts_branch .Lse_acc\n"
      "\ts_waitcnt vmcnt(2)\n"
      "\ts_branch .Lse_acc\n"
      "\ts_waitcnt vmcnt(3)\n"
      "\ts_branch .Lse_acc\n"
      "\ts_waitcnt vmcnt(4)\n"
      "\ts_branch .Lse_acc\n"
      "\ts_waitcnt vmcnt(5)\n"
      "\ts_branch .Lse_acc\n"
      "\ts_waitcnt vmcnt(6)\n"
      "\ts_branch .Lse_acc\n"
      "\ts_waitcnt vmcnt(7)\n"
      "\ts_branch .Lse_acc\n"
      "\ts_waitcnt vmcnt(8)\n"
      "\ts_branch .Lse_acc\n"
      "\ts_waitcnt vmcnt(9)\n"
      "\ts_branch .Lse_acc\n"
      "\ts_waitcnt vmcnt(10)\n"
      "\ts_branch .Lse_acc\n"
      "\ts_waitcnt vmcnt(11)\n"
      "\ts_branch .Lse_acc\n"
      "\ts_waitcnt vmcnt(12)\n"
      "\ts_branch .Lse_acc\n"
      "\ts_waitcnt vmcnt(13)\n"
      "\ts_branch .Lse_acc\n"
      "\ts_waitcnt vmcnt(14)\n"
      "\ts_branch .Lse_acc\n"
      "\ts_waitcnt vmcnt(15)\n"
      "\ts_branch .Lse_acc\n"
      "\ts_waitcnt vmcnt(16)\n"
      "\ts_branch .Lse_acc\n"
      "\ts_waitcnt vmcnt(17)\n"
      "\ts_branch .Lse_acc\n"
      "\ts_waitcnt vmcnt(18)\n"
      "\ts_branch .Lse_acc\n"
      "\ts_waitcnt vmcnt(19)\n"
      "\ts_branch .Lse_acc\n"
      "\ts_waitcnt vmcnt(20)\n"
      "\ts_branch .Lse_acc\n"
      "\ts_waitcnt vmcnt(21)\n"
      "\ts_branch .Lse_acc\n"
      "\ts_waitcnt vmcnt(22)\n"
      "\ts_branch .Lse_acc\n"
      "\ts_waitcnt vmcnt(23)\n"
      "\ts_branch .Lse_acc\n"
      "\ts_waitcnt vmcnt(24)\n"
      "\ts_branch .Lse_acc\n"
      "\ts_waitcnt vmcnt(25)\n"
      "\ts_branch .Lse_acc\n"
      "\ts_waitcnt vmcnt(26)\n"
      "\ts_branch .Lse_acc\n"
      "\ts_waitcnt vmcnt(27)\n"
      "\ts_branch .Lse_acc\n"
      "\ts_waitcnt vmcnt(28)\n"
      "\ts_branch .Lse_acc\n"
      "\ts_waitcnt vmcnt(29)\n"
      "\ts_branch .Lse_acc\n"
      "\ts_waitcnt vmcnt(30)\n"
      "\ts_branch .Lse_acc\n"
      "\ts_waitcnt vmcnt(31)\n"
      "\ts_branch .Lse_acc\n"
      ".Lse_cpf:\n"
      "\ts_mov_b32 s30, 0\n"
      ".Lse_cpl:\n"
      "\ts_load_dword s19, s[22:23], s30\n"
      "\ts_addk_i32 s30, 0x80\n"
      "\ts_cmp_lt_u32 s30, .Lse_ret-.Lse_tab\n"
      "\ts_cbranch_scc1 .Lse_cpl\n"
      "\ts_setpc_b64 s[92:93]\n"
      ".Lse_stg:\n"
      "\ts_cmp_eq_u32 s24, 8\n"
      "\ts_cbranch_scc0 .Lse_i1a\n"
      "\tbuffer_load_dword v147, off, s[60:63], 0 sc1\n"
      ".Lse_i1a:\n"
      "\ts_sub_u32 s31, s24, 9\n"
      "\ts_cmp_lt_u32 s31, 2\n"
      "\ts_cbranch_scc0 .Lse_i1b\n"
      "\ts_cmp_eq_u32 s31, 0\n"
      "\ts_cselect_b64 s[96:97], s[8:9], s[10:11]\n"
      "\ts_pack_ll_b32_b16 s97, s97, 0\n"
      "\ts_movk_i32 s98, 4608\n"
      "\ts_mov_b32 s99, s67\n"
      "\ts_movk_i32 s36, 0x1000\n"
      "\tbuffer_load_dwordx4 v[148:151], v14, s[96:99], 0 offen offset:0\n"
      "\tbuffer_load_dwordx4 v[152:155], v14, s[96:99], 0 offen offset:1024\n"
      "\tbuffer_load_dwordx4 v[156:159], v14, s[96:99], 0 offen offset:2048\n"
      "\tbuffer_load_dwordx4 v[160:163], v14, s[96:99], 0 offen offset:3072\n"
      "\tbuffer_load_dwordx4 v[164:167], v14, s[96:99], s36 offen\n"
      ".Lse_i1b:\n"
      "\ts_setpc_b64 s[92:93]\n"
      ".Lse_main:\n"
      "\tbuffer_load_dwordx4 v[44:47], v15, s[64:67], s68 offen nt\n"
      "\tv_and_b32 v1, 63, v0\n"
      "\ts_mov_b64 s[60:61], s[14:15]\n"
      "\ts_pack_ll_b32_b16 s61, s61, 0\n"
      "\ts_mov_b32 s62, 655360\n"
      "\ts_mov_b32 s63, s67\n"
      "\tv_lshlrev_b32 v14, 4, v1\n"
      "\ts_lshr_b32 s31, %[b], 3\n"
      "\ts_xor_b32 s31, s31, 16\n"
      "\ts_or_b32 s31, s31, s24\n"
      "\ts_cbranch_scc1 .Lse_i1t\n"
      "\ts_and_b32 s31, %[b], 7\n"
      "\ts_lshl_b32 s31, s31, 2\n"
      "\tv_mov_b32 v24, s71\n"
      "\ts_mov_b64 exec, 1\n"
      "\tbuffer_store_dword v24, off, s[60:63], s31 offset:2048 sc1\n"
      "\ts_mov_b64 exec, -1\n"
      ".Lse_i1t:\n"
      "\ts_lshr_b32 s30, %[b], 4\n"
      "\ts_xor_b32 s31, s24, 5\n"
      "\ts_or_b32 s30, s30, s31\n"
      "\ts_cbranch_scc1 .Lse_wt\n"
      "\ts_load_dword s30, s[14:15], 0x400\n"
      "\ts_load_dword s31, s[8:9], 0x0\n"
      "\ts_load_dword s19, s[10:11], 0x0\n"
      "\ts_waitcnt lgkmcnt(0)\n"
      ".Lse_wt:\n"
      "\ts_bfe_u32 s7, %[b], 0x40003\n"
      "\ts_add_u32 s7, s7, 4\n"
      "\ts_sub_u32 s31, s24, 8\n"
      "\ts_cmp_lt_u32 s31, 3\n"
      "\ts_cselect_b32 s7, s7, 99\n"
      "\ts_cmp_eq_u32 s24, 4\n"
      "\ts_cbranch_scc0 .Lse_x5\n"
      "\ts_lshr_b32 s31, %[b], 3\n"
      "\ts_mul_i32 s31, s31, 0x300000\n"
      "\ts_add_u32 s34, s64, s31\n"
      "\ts_addc_u32 s35, s65, 0\n"
      "\ts_load_dword s30, s[34:35], 0x0\n"
      "\ts_add_u32 s34, s34, 0x180000\n"
      "\ts_addc_u32 s35, s35, 0\n"
      "\ts_load_dword s19, s[34:35], 0x0\n"
      "\ts_waitcnt lgkmcnt(0)\n"
      ".Lse_x5:\n"
      "\ts_call_b64 s[76:77], .Lse_sub\n"
      "\tbuffer_load_dwordx4 v[48:51], v15, s[64:67], s68 offen nt\n"
      "\ts_mov_b64 s[26:27], -1\n"
      "\tv_mov_b64 v[24:25], 0\n"
      "\tv_mov_b64 v[26:27], 0\n"
      "\tds_write_b128 v14, v[24:27] offset:36096\n"
      "\tv_mul_u32_u24 v10, 0xaab, v0\n"
      "\tv_lshrrev_b32 v10, 16, v10\n"
      "\tv_mul_u32_u24 v13, 24, v10\n"
      "\tv_sub_u32 v13, v0, v13\n"
      "\tv_lshlrev_b32 v13, 5, v13\n"
      "\tv_mul_u32_u24 v10, 0x320, v10\n"
      "\tv_add_u32 v10, v10, v13\n"
      "\ts_call_b64 s[76:77], .Lse_sub\n"
      "\tbuffer_load_dwordx4 v[52:55], v15, s[64:67], s68 offen nt\n"
      "\tv_lshrrev_b32 v24, 3, v1\n"
      "\tv_mul_u32_u24 v11, 0xc80, v24\n"
      "\tv_and_b32 v25, 7, v1\n"
      "\tv_lshl_add_u32 v11, v25, 3, v11\n"
      "\ts_lshl_b32 s31, s24, 6\n"
      "\tv_add_u32 v11, s31, v11\n"
      "\tv_lshrrev_b32 v26, 2, v25\n"
      "\tv_lshlrev_b32 v12, 14, v26\n"
      "\tv_and_b32 v26, 3, v1\n"
      "\tv_lshl_add_u32 v12, v26, 4, v12\n"
      "\tv_lshrrev_b32 v26, 5, v1\n"
      "\tv_lshl_add_u32 v12, v26, 3, v12\n"
      "\ts_lshl_b32 s31, s24, 15\n"
      "\tv_add_u32 v12, s31, v12\n"
      "\tv_and_b32 v26, 31, v1\n"
      "\tv_cmp_gt_u32 vcc, 8, v26\n"
      "\tv_mov_b32 v27, 2147483632\n"
      "\ts_nop 0\n"
      "\tv_cndmask_b32 v12, v27, v12, vcc\n"
      "\ts_call_b64 s[76:77], .Lse_sub\n"
      "\tbuffer_load_dwordx4 v[56:59], v15, s[64:67], s68 offen nt\n"
      "\ts_cmp_eq_u32 s7, 4\n"
      "\ts_cbranch_scc0 .Lse_sg4\n"
      "\ts_call_b64 s[92:93], .Lse_stg\n"
      ".Lse_sg4:\n"
      "\ts_lshl_b32 s29, %[b], 6\n"
      "\ts_add_u32 s29, s29, 262144\n"
      "\ts_lshl_b32 s37, %[b], 14\n"
      "\ts_lshl_b32 s31, s24, 11\n"
      "\ts_add_u32 s37, s37, s31\n"
      "\ts_add_u32 s37, s37, 253952\n"
      "\ts_lshl_b32 s38, %[b], 7\n"
      "\ts_add_u32 s38, s38, 4096\n"
      "\ts_add_u32 s52, s38, 17408\n"
      "\ts_and_b32 s39, %[b], 7\n"
      "\ts_mul_i32 s39, s39, 4352\n"
      "\ts_add_u32 s39, s39, 4096\n"
      "\ts_mov_b32 s40, 15\n"
      "\ts_mov_b32 s41, 15\n"
      "\ts_mov_b32 s42, 0x36800000\n"
      "\ts_mov_b32 s43, s42\n"
      "\ts_call_b64 s[76:77], .Lse_sub\n"
      "\tbuffer_load_dwordx4 v[60:63], v15, s[64:67], s68 offen nt\n"
      "\ts_cmp_eq_u32 s7, 5\n"
      "\ts_cbranch_scc0 .Lse_sg5\n"
      "\ts_call_b64 s[92:93], .Lse_stg\n"
      ".Lse_sg5:\n"
      "\ts_mov_b32 s44, -1\n"
      "\ts_mov_b32 s45, 0xffff\n"
      "\ts_mov_b32 s48, 0\n"
      "\ts_brev_b32 s49, 1\n"
      "\ts_mov_b64 s[80:81], s[12:13]\n"
      "\ts_pack_ll_b32_b16 s81, s81, 0\n"
      "\ts_mov_b32 s82, s66\n"
      "\ts_mov_b32 s83, s67\n"
      "\ts_mul_i32 s86, s24, 3353258\n"
      "\ts_lshr_b32 s86, s86, 16\n"
      "\ts_xor_b32 s50, s24, 11\n"
      "\ts_or_b32 s50, s50, %[b]\n"
      "\ts_mov_b32 s51, %[b]\n"
      "\ts_call_b64 s[76:77], .Lse_sub\n"
      "\tbuffer_load_dwordx4 v[64:67], v15, s[64:67], s68 offen nt\n"
      "\ts_cmp_eq_u32 s7, 6\n"
      "\ts_cbranch_scc0 .Lse_sg6\n"
      "\ts_call_b64 s[92:93], .Lse_stg\n"
      ".Lse_sg6:\n"
      "\ts_call_b64 s[76:77], .Lse_sub\n"
      "\tbuffer_load_dwordx4 v[68:71], v15, s[64:67], s68 offen nt\n"
      "\ts_cmp_eq_u32 s7, 7\n"
      "\ts_cbranch_scc0 .Lse_sg7\n"
      "\ts_call_b64 s[92:93], .Lse_stg\n"
      ".Lse_sg7:\n"
      "\ts_cmp_eq_u32 s24, 7\n"
      "\ts_cbranch_scc0 .Lse_t1\n"
      "\tv_and_b32 v24, 7, v1\n"
      "\tv_lshlrev_b32 v24, 2, v24\n"
      "\tbuffer_load_dword v140, v24, s[60:63], 0 offen offset:2048 sc1\n"
      "\ts_lshr_b32 s17, s72, 4\n"
      ".Lse_t1:\n"
      "\ts_call_b64 s[76:77], .Lse_sub\n"
      "\tbuffer_load_dwordx4 v[72:75], v15, s[64:67], s68 offen nt\n"
      "\ts_cmp_eq_u32 s24, 2\n"
      "\ts_cselect_b32 s31, 2, 0\n"
      "\ts_mul_i32 s31, s31, s79\n"
      "\ts_sub_i32 s70, s70, s31\n"
      "\ts_cmp_eq_u32 s7, 8\n"
      "\ts_cbranch_scc0 .Lse_sg8\n"
      "\ts_call_b64 s[92:93], .Lse_stg\n"
      ".Lse_sg8:\n"
      "\ts_call_b64 s[76:77], .Lse_sub\n"
      "\tbuffer_load_dwordx4 v[76:79], v15, s[64:67], s68 offen nt\n"
      "\ts_cmp_eq_u32 s7, 9\n"
      "\ts_cbranch_scc0 .Lse_sg9\n"
      "\ts_call_b64 s[92:93], .Lse_stg\n"
      ".Lse_sg9:\n"
      "\ts_call_b64 s[76:77], .Lse_sub\n"
      "\tbuffer_load_dwordx4 v[80:83], v15, s[64:67], s68 offen nt\n"
      "\ts_cmp_eq_u32 s7, 10\n"
      "\ts_cbranch_scc0 .Lse_sg10\n"
      "\ts_call_b64 s[92:93], .Lse_stg\n"
      ".Lse_sg10:\n"
      "\ts_cmp_eq_u32 s24, 2\n"
      "\ts_cbranch_scc0 .Lse_x6\n"
      "\ts_lshr_b32 s31, %[b], 3\n"
      "\ts_mul_i32 s31, s31, 0x300000\n"
      "\ts_add_u32 s34, s80, s31\n"
      "\ts_addc_u32 s35, s81, 0\n"
      "\ts_load_dword s30, s[34:35], 0x0\n"
      "\ts_add_u32 s34, s34, 0x180000\n"
      "\ts_addc_u32 s35, s35, 0\n"
      "\ts_load_dword s19, s[34:35], 0x0\n"
      "\ts_waitcnt lgkmcnt(0)\n"
      ".Lse_x6:\n"
      "\ts_cmp_eq_u32 s24, 2\n"
      "\ts_cselect_b32 s31, 2, 0\n"
      "\ts_mul_i32 s31, s31, s79\n"
      "\ts_add_i32 s70, s70, s31\n"
      "\ts_call_b64 s[76:77], .Lse_sub\n"
      "\tbuffer_load_dwordx4 v[84:87], v15, s[64:67], s68 offen nt\n"
      "\ts_cmp_eq_u32 s7, 11\n"
      "\ts_cbranch_scc0 .Lse_sg11\n"
      "\ts_call_b64 s[92:93], .Lse_stg\n"
      ".Lse_sg11:\n"
      "\ts_cmp_eq_u32 s24, 7\n"
      "\ts_cbranch_scc0 .Lse_t2\n"
      "\ts_waitcnt vmcnt(4)\n"
      "\tv_subrev_u32 v24, s71, v140\n"
      "\ts_sub_i32 s31, s17, 100\n"
      "\tv_mov_b32 v25, s31\n"
      "\tv_cmp_ge_i32 vcc, s17, v24\n"
      "\ts_movk_i32 s31, -400\n"
      "\tv_cmp_le_i32 s[46:47], s31, v24\n"
      "\ts_and_b64 vcc, vcc, s[46:47]\n"
      "\ts_nop 1\n"
      "\tv_cndmask_b32 v24, v25, v24, vcc\n"
      "\ts_nop 1\n"
      "\tv_add_u32_dpp v24, v24, v24 quad_perm:[1,0,3,2] row_mask:0xf bank_mask:0xf\n"
      "\ts_nop 1\n"
      "\tv_add_u32_dpp v24, v24, v24 quad_perm:[2,3,0,1] row_mask:0xf bank_mask:0xf\n"
      "\ts_nop 1\n"
      "\tv_add_u32_dpp v24, v24, v24 row_ror:4 row_mask:0xf bank_mask:0xf\n"
      "\tv_ashrrev_i32 v24, 3, v24\n"
      "\tv_add_u32 v24, s71, v24\n"
      "\tv_mov_b32 v25, 0\n"
      "\tds_write_b32 v25, v24 offset:37956\n"
      ".Lse_t2:\n"
      "\ts_call_b64 s[76:77], .Lse_sub\n"
      "\tbuffer_load_dwordx4 v[88:91], v15, s[64:67], s68 offen nt\n"
      "\ts_cmp_eq_u32 s7, 12\n"
      "\ts_cbranch_scc0 .Lse_sg12\n"
      "\ts_call_b64 s[92:93], .Lse_stg\n"
      ".Lse_sg12:\n"
      "\tv_mov_b32 v24, 0\n"
      "\tds_read_b32 v25, v24 offset:37956\n"
      "\ts_waitcnt lgkmcnt(0)\n"
      "\tv_readfirstlane_b32 s17, v25\n"
      "\ts_sub_i32 s17, s17, s71\n"
      "\ts_add_i32 s31, s17, 400\n"
      "\ts_cmpk_lt_u32 s31, 800\n"
      "\ts_cselect_b32 s17, s17, 0\n"
      "\ts_movk_i32 s31, -150\n"
      "\ts_max_i32 s17, s17, s31\n"
      "\ts_movk_i32 s31, 150\n"
      "\ts_min_i32 s17, s17, s31\n"
      "\ts_add_u32 s5, s71, s17\n"
      "\ts_add_u32 s6, s5, 1514\n"
      "\ts_add_u32 s5, s5, 1411\n"
      "\ts_mul_i32 s31, s24, 3\n"
      "\ts_add_u32 s5, s5, s31\n"
      "\ts_mul_i32 s17, s17, 3449\n"
      "\ts_ashr_i32 s17, s17, 12\n"
      "\ts_add_i32 s79, s79, s17\n"
      "\ts_call_b64 s[76:77], .Lse_sub\n"
      "\tbuffer_load_dwordx4 v[92:95], v15, s[64:67], s68 offen nt\n"
      "\ts_cmp_eq_u32 s24, 5\n"
      "\ts_cselect_b32 s31, 2, 0\n"
      "\ts_mul_i32 s31, s31, s79\n"
      "\ts_sub_i32 s70, s70, s31\n"
      "\ts_cmp_eq_u32 s7, 13\n"
      "\ts_cbranch_scc0 .Lse_sg13\n"
      "\ts_call_b64 s[92:93], .Lse_stg\n"
      ".Lse_sg13:\n"
      "\ts_call_b64 s[76:77], .Lse_sub\n"
      "\tbuffer_load_dwordx4 v[96:99], v15, s[64:67], s68 offen nt\n"
      "\ts_cmp_eq_u32 s24, 0\n"
      "\ts_cselect_b32 s31, 2, 0\n"
      "\ts_mul_i32 s31, s31, s79\n"
      "\ts_sub_i32 s70, s70, s31\n"
      "\ts_cmp_eq_u32 s7, 14\n"
      "\ts_cbranch_scc0 .Lse_sg14\n"
      "\ts_call_b64 s[92:93], .Lse_stg\n"
      ".Lse_sg14:\n"
      "\ts_call_b64 s[76:77], .Lse_sub\n"
      "\tbuffer_load_dwordx4 v[100:103], v15, s[64:67], s68 offen nt\n"
      "\ts_cmp_eq_u32 s7, 15\n"
      "\ts_cbranch_scc0 .Lse_sg15\n"
      "\ts_call_b64 s[92:93], .Lse_stg\n"
      ".Lse_sg15:\n"
      "\ts_cmp_eq_u32 s24, 5\n"
      "\ts_cbranch_scc0 .Lse_r0\n"
      "\ts_mov_b64 exec, 0\n"
      "\ts_mov_b64 s[26:27], 0\n"
      "\ts_call_b64 s[92:93], .Lse_seg1\n"
      "\ts_mov_b64 exec, -1\n"
      "\ts_mov_b64 s[26:27], -1\n"
      ".Lse_r0:\n"
      "\ts_cmp_eq_u32 s24, 5\n"
      "\ts_cselect_b32 s31, 2, 0\n"
      "\ts_mul_i32 s31, s31, s79\n"
      "\ts_add_i32 s70, s70, s31\n"
      "\ts_call_b64 s[76:77], .Lse_sub\n"
      "\tbuffer_load_dwordx4 v[104:107], v15, s[64:67], s68 offen nt\n"
      "\ts_cmp_eq_u32 s24, 2\n"
      "\ts_cselect_b32 s31, 2, 0\n"
      "\ts_mul_i32 s31, s31, s79\n"
      "\ts_sub_i32 s70, s70, s31\n"
      "\ts_cmp_eq_u32 s7, 16\n"
      "\ts_cbranch_scc0 .Lse_sg16\n"
      "\ts_call_b64 s[92:93], .Lse_stg\n"
      ".Lse_sg16:\n"
      "\ts_cmp_eq_u32 s24, 0\n"
      "\ts_cbranch_scc0 .Lse_r1\n"
      "\ts_mov_b64 exec, 0\n"
      "\ts_mov_b64 s[26:27], 0\n"
      "\ts_call_b64 s[92:93], .Lse_seg2\n"
      "\ts_mov_b64 exec, -1\n"
      "\ts_mov_b64 s[26:27], -1\n"
      ".Lse_r1:\n"
      "\ts_cmp_eq_u32 s24, 0\n"
      "\ts_cselect_b32 s31, 2, 0\n"
      "\ts_mul_i32 s31, s31, s79\n"
      "\ts_add_i32 s70, s70, s31\n"
      "\ts_call_b64 s[76:77], .Lse_sub\n"
      "\tbuffer_load_dwordx4 v[108:111], v15, s[64:67], s68 offen nt\n"
      "\ts_cmp_eq_u32 s24, 3\n"
      "\ts_cselect_b32 s31, 2, 0\n"
      "\ts_mul_i32 s31, s31, s79\n"
      "\ts_sub_i32 s70, s70, s31\n"
      "\ts_cmp_eq_u32 s7, 17\n"
      "\ts_cbranch_scc0 .Lse_sg17\n"
      "\ts_call_b64 s[92:93], .Lse_stg\n"
      ".Lse_sg17:\n"
      "\ts_call_b64 s[76:77], .Lse_sub\n"
      "\tbuffer_load_dwordx4 v[112:115], v15, s[64:67], s68 offen nt\n"
      "\ts_cmp_eq_u32 s7, 18\n"
      "\ts_cbranch_scc0 .Lse_sg18\n"
      "\ts_call_b64 s[92:93], .Lse_stg\n"
      ".Lse_sg18:\n"
      "\ts_cmp_eq_u32 s24, 2\n"
      "\ts_cbranch_scc0 .Lse_r2\n"
      "\ts_mov_b64 exec, 0\n"
      "\ts_mov_b64 s[26:27], 0\n"
      "\ts_call_b64 s[92:93], .Lse_seg3\n"
      "\ts_mov_b64 exec, -1\n"
      "\ts_mov_b64 s[26:27], -1\n"
      ".Lse_r2:\n"
      "\ts_cmp_eq_u32 s24, 2\n"
      "\ts_cselect_b32 s31, 2, 0\n"
      "\ts_mul_i32 s31, s31, s79\n"
      "\ts_add_i32 s70, s70, s31\n"
      "\ts_call_b64 s[76:77], .Lse_sub\n"
      "\tbuffer_load_dwordx4 v[116:119], v15, s[64:67], s68 offen nt\n"
      "\ts_cmp_eq_u32 s24, 1\n"
      "\ts_cselect_b32 s31, 2, 0\n"
      "\ts_mul_i32 s31, s31, s79\n"
      "\ts_sub_i32 s70, s70, s31\n"
      "\ts_cmp_eq_u32 s7, 19\n"
      "\ts_cbranch_scc0 .Lse_sg19\n"
      "\ts_call_b64 s[92:93], .Lse_stg\n"
      ".Lse_sg19:\n"
      "\ts_cmp_eq_u32 s24, 3\n"
      "\ts_cbranch_scc0 .Lse_r3\n"
      "\ts_mov_b64 exec, 0\n"
      "\ts_mov_b64 s[26:27], 0\n"
      "\ts_call_b64 s[92:93], .Lse_seg4\n"
      "\ts_mov_b64 exec, -1\n"
      "\ts_mov_b64 s[26:27], -1\n"
      ".Lse_r3:\n"
      "\ts_cmp_eq_u32 s24, 3\n"
      "\ts_cselect_b32 s31, 2, 0\n"
      "\ts_mul_i32 s31, s31, s79\n"
      "\ts_add_i32 s70, s70, s31\n"
      "\ts_call_b64 s[76:77], .Lse_sub\n"
      "\tbuffer_load_dwordx4 v[120:123], v15, s[64:67], s68 offen nt\n"
      "\ts_cmp_eq_u32 s24, 6\n"
      "\ts_cselect_b32 s31, 2, 0\n"
      "\ts_mul_i32 s31, s31, s79\n"
      "\ts_sub_i32 s70, s70, s31\n"
      "\ts_call_b64 s[76:77], .Lse_sub\n"
      "\tbuffer_load_dwordx4 v[124:127], v15, s[64:67], s68 offen nt\n"
      "\ts_cmp_eq_u32 s24, 1\n"
      "\ts_cbranch_scc0 .Lse_r4\n"
      "\ts_mov_b64 exec, 0\n"
      "\ts_mov_b64 s[26:27], 0\n"
      "\ts_call_b64 s[92:93], .Lse_seg5\n"
      "\ts_mov_b64 exec, -1\n"
      "\ts_mov_b64 s[26:27], -1\n"
      ".Lse_r4:\n"
      "\ts_cmp_eq_u32 s24, 1\n"
      "\ts_cselect_b32 s31, 2, 0\n"
      "\ts_mul_i32 s31, s31, s79\n"
      "\ts_add_i32 s70, s70, s31\n"
      "\ts_call_b64 s[76:77], .Lse_sub\n"
      "\tbuffer_load_dwordx4 v[128:131], v15, s[64:67], s68 offen nt\n"
      "\ts_cmp_eq_u32 s24, 6\n"
      "\ts_cbranch_scc0 .Lse_r5\n"
      "\ts_mov_b64 exec, 0\n"
      "\ts_mov_b64 s[26:27], 0\n"
      "\ts_call_b64 s[92:93], .Lse_seg0\n"
      "\ts_mov_b64 exec, -1\n"
      "\ts_mov_b64 s[26:27], -1\n"
      ".Lse_r5:\n"
      "\ts_cmp_eq_u32 s24, 6\n"
      "\ts_cselect_b32 s31, 2, 0\n"
      "\ts_mul_i32 s31, s31, s79\n"
      "\ts_add_i32 s70, s70, s31\n"
      "\ts_call_b64 s[76:77], .Lse_sub\n"
      "\tbuffer_load_dwordx4 v[132:135], v15, s[64:67], s68 offen nt\n"
      "\ts_call_b64 s[76:77], .Lse_sub\n"
      "\tbuffer_load_dwordx4 v[136:139], v15, s[64:67], s68 offen nt\n"
      "\ts_call_b64 s[76:77], .Lse_sub\n"
      "\tbuffer_load_dwordx4 v[140:143], v15, s[64:67], s68 offen nt\n"
      "\ts_cmp_eq_u32 s24, 8\n"
      "\ts_cbranch_scc0 .Lse_i8a\n"
      "\ts_waitcnt vmcnt(5)\n"
      "\tv_mov_b32 v24, 0\n"
      "\tds_write_b32 v24, v147 offset:37952\n"
      ".Lse_i8a:\n"
      "\ts_sub_u32 s31, s24, 9\n"
      "\ts_cmp_lt_u32 s31, 2\n"
      "\ts_cbranch_scc0 .Lse_i8b\n"
      "\ts_mul_i32 s31, s31, 5120\n"
      "\tv_add_u32 v24, s31, v14\n"
      "\ts_waitcnt vmcnt(5)\n"
      "\tds_write_b128 v24, v[148:151] offset:25600\n"
      "\tds_write_b128 v24, v[152:155] offset:26624\n"
      "\tds_write_b128 v24, v[156:159] offset:27648\n"
      "\tds_write_b128 v24, v[160:163] offset:28672\n"
      "\tds_write_b128 v24, v[164:167] offset:29696\n"
      ".Lse_i8b:\n"
      "\ts_call_b64 s[76:77], .Lse_sub\n"
      "\tbuffer_load_dwordx4 v[144:147], v15, s[64:67], s68 offen nt\n"
      "\ts_call_b64 s[76:77], .Lse_sub\n"
      "\tbuffer_load_dwordx4 v[148:151], v15, s[64:67], s68 offen nt\n"
      "\ts_call_b64 s[76:77], .Lse_sub\n"
      "\tbuffer_load_dwordx4 v[152:155], v15, s[64:67], s68 offen nt\n"
      "\ts_call_b64 s[76:77], .Lse_sub\n"
      "\tbuffer_load_dwordx4 v[156:159], v15, s[64:67], s68 offen nt\n"
      "\ts_call_b64 s[76:77], .Lse_sub\n"
      "\tbuffer_load_dwordx4 v[160:163], v15, s[64:67], s68 offen nt\n"
      "\ts_call_b64 s[76:77], .Lse_sub\n"
      "\tbuffer_load_dwordx4 v[164:167], v15, s[64:67], s68 offen nt\n"
      "\ts_mov_b32 s79, 0\n"
      "\ts_add_u32 s94, s94, 1\n"
      ".Lse_tl:\n"
      "\ts_cmp_le_u32 s94, 1\n"
      "\ts_cbranch_scc1 .Lse_tw\n"
      "\ts_call_b64 s[76:77], .Lse_accj\n"
      "\ts_branch .Lse_tl\n"
      ".Lse_tw:\n"
      "\ts_waitcnt vmcnt(0)\n"
      ".Lse_seg0:\n"
      "\tv_lshlrev_b32 v24, 16, v164\n"
      "\tv_and_b32 v25, s75, v164\n"
      "\tv_lshlrev_b32 v26, 16, v165\n"
      "\tv_and_b32 v27, s75, v165\n"
      "\tv_lshlrev_b32 v28, 16, v166\n"
      "\tv_and_b32 v29, s75, v166\n"
      "\tv_lshlrev_b32 v30, 16, v167\n"
      "\tv_and_b32 v31, s75, v167\n"
      "\tv_pk_add_f32 v[2:3], v[2:3], v[24:25]\n"
      "\tv_pk_add_f32 v[4:5], v[4:5], v[26:27]\n"
      "\tv_pk_add_f32 v[6:7], v[6:7], v[28:29]\n"
      "\tds_write_b128 v10, v[2:5]\n"
      "\tv_pk_add_f32 v[8:9], v[8:9], v[30:31]\n"
      "\ts_nop 0\n"
      "\tds_write_b128 v10, v[6:9] offset:16\n"
      "\ts_waitcnt lgkmcnt(0)\n"
      "\tv_mov_b32 v8, 0\n"
      "\ts_cbranch_execz .Lse_ret\n"
      "\ts_barrier\n"
      ".Lse_seg1:\n"
      "\tds_read_b32 v33, v8 offset:37952\n"
      "\tds_read_b64 v[24:25], v11 offset:0\n"
      "\tds_read_b64 v[26:27], v11 offset:800\n"
      "\tds_read_b64 v[28:29], v11 offset:1600\n"
      "\tds_read_b64 v[30:31], v11 offset:2400\n"
      "\ts_waitcnt lgkmcnt(2)\n"
      "\tv_pk_add_f32 v[24:25], v[24:25], v[26:27]\n"
      "\tv_add_u32 v33, 1, v33\n"
      "\ts_waitcnt lgkmcnt(0)\n"
      "\tv_pk_add_f32 v[28:29], v[28:29], v[30:31]\n"
      "\tv_max_u32 v31, 1, v33\n"
      "\tv_pk_add_f32 v[24:25], v[24:25], v[28:29]\n"
      "\ts_nop 1\n"
      "\tv_permlane32_swap_b32 v24, v25\n"
      "\tv_add_f32 v24, v24, v25\n"
      "\tv_readfirstlane_b32 s28, v31\n"
      "\ts_nop 0\n"
      "\tv_add_f32_dpp v24, v24, v24 row_ror:8 row_mask:0xf bank_mask:0xf\n"
      "\tv_mov_b32 v26, v24\n"
      "\ts_nop 1\n"
      "\tv_permlane16_swap_b32 v24, v26\n"
      "\tv_add_f32 v30, v24, v26\n"
      "\ts_cbranch_execz .Lse_p1\n"
      "\tbuffer_store_dwordx2 v[30:31], v12, s[60:63], s29 offen sc1\n"
      ".Lse_p1:\n"
      "\tv_mov_b64 v[16:17], 0\n"
      "\tv_mov_b64 v[18:19], 0\n"
      "\tv_mov_b64 v[20:21], 0\n"
      "\tv_mov_b64 v[22:23], 0\n"
      "\tv_mov_b64 v[32:33], 0\n"
      "\tv_mov_b64 v[34:35], 0\n"
      "\tv_mov_b64 v[36:37], 0\n"
      "\tv_mov_b64 v[38:39], 0\n"
      "\tv_lshlrev_b32 v24, 1, v0\n"
      "\tds_read_u16_d16_hi v16, v24 offset:30720\n"
      "\tds_read_u16_d16_hi v17, v24 offset:31104\n"
      "\tds_read_u16_d16_hi v18, v24 offset:31488\n"
      "\tds_read_u16_d16_hi v19, v24 offset:31872\n"
      "\tds_read_u16_d16_hi v20, v24 offset:32256\n"
      "\tds_read_u16_d16_hi v21, v24 offset:32640\n"
      "\tds_read_u16_d16_hi v22, v24 offset:33024\n"
      "\tds_read_u16_d16_hi v23, v24 offset:33408\n"
      "\tds_read_u16_d16_hi v32, v24 offset:33792\n"
      "\tds_read_u16_d16_hi v33, v24 offset:34176\n"
      "\tds_read_u16_d16_hi v34, v24 offset:34560\n"
      "\tds_read_u16_d16_hi v35, v24 offset:34944\n"
      "\tv_mul_u32_u24 v25, 0x60, v1\n"
      "\ts_lshl_b32 s31, s24, 1\n"
      "\tv_add_u32 v25, s31, v25\n"
      "\ts_waitcnt lgkmcnt(4)\n"
      "\ts_and_b64 exec, s[26:27], s[44:45]\n"
      "\tds_read_u16_d16_hi v36, v25 offset:25600\n"
      "\tds_read_u16_d16_hi v37, v25 offset:25624\n"
      "\tds_read_u16_d16_hi v38, v25 offset:25648\n"
      "\tds_read_u16_d16_hi v39, v25 offset:25672\n"
      "\ts_mov_b64 exec, s[26:27]\n"
      "\tv_lshrrev_b32 v2, 4, v1\n"
      "\tv_mul_u32_u24 v2, 4352, v2\n"
      "\tv_and_b32 v3, 15, v1\n"
      "\tv_lshl_add_u32 v2, v3, 3, v2\n"
      "\tv_and_b32 v3, 31, v1\n"
      "\tv_lshlrev_b32 v3, 3, v3\n"
      "\tv_lshrrev_b32 v4, 1, v1\n"
      "\tv_lshlrev_b32 v4, 7, v4\n"
      "\tv_and_b32 v5, 1, v1\n"
      "\tv_lshl_add_u32 v4, v5, 4, v4\n"
      "\tv_and_b32 v5, 3, v1\n"
      "\tv_lshlrev_b32 v5, 3, v5\n"
      "\tv_lshrrev_b32 v6, 5, v1\n"
      "\tv_lshl_add_u32 v5, v6, 2, v5\n"
      "\ts_lshl_b32 s31, s24, 5\n"
      "\tv_add_u32 v5, s31, v5\n"
      "\tv_lshlrev_b32 v6, 2, v0\n"
      "\ts_lshl_b32 s31, s24, 2\n"
      "\tv_mov_b32 v7, s31\n"
      "\ts_cmp_lt_u32 s51, 24\n"
      "\ts_cbranch_scc0 .Lse_b4\n"
      "\ts_cmp_lt_u32 s24, 4\n"
      "\ts_cbranch_scc1 .Lse_b2e\n"
      "\ts_mov_b32 s30, 0\n"
      "\ts_cbranch_execz .Lse_b2x\n"
      "\ts_mov_b32 s31, 0\n"
      ".Lse_tw1:\n"
      "\ts_memrealtime s[88:89]\n"
      "\ts_add_u32 s31, s31, 1\n"
      "\ts_waitcnt lgkmcnt(0)\n"
      "\ts_sub_u32 s88, s88, s5\n"
      "\ts_cmp_ge_i32 s88, 0\n"
      "\ts_cbranch_scc1 .Lse_tw1x\n"
      "\ts_cmp_lt_u32 s31, 0x400\n"
      "\ts_cbranch_scc1 .Lse_tw1\n"
      ".Lse_tw1x:\n"
      ".Lse_b2l:\n"
      "\tbuffer_load_dwordx4 v[24:27], v14, s[60:63], s37 offen sc1\n"
      "\tbuffer_load_dwordx4 v[28:31], v14, s[60:63], s37 offen offset:1024 sc1\n"
      "\ts_add_u32 s30, s30, 1\n"
      "\ts_waitcnt vmcnt(1)\n"
      "\tv_cmp_ne_u32 vcc, s28, v25\n"
      "\tv_cmp_ne_u32 s[46:47], s28, v27\n"
      "\ts_or_b64 s[90:91], vcc, s[46:47]\n"
      "\ts_waitcnt vmcnt(0)\n"
      "\tv_cmp_ne_u32 vcc, s28, v29\n"
      "\tv_cmp_ne_u32 s[46:47], s28, v31\n"
      "\ts_or_b64 s[90:91], s[90:91], vcc\n"
      "\ts_or_b64 s[90:91], s[90:91], s[46:47]\n"
      "\ts_cbranch_scc0 .Lse_b2x\n"
      "\ts_cmp_lt_u32 s30, 0x40000\n"
      "\ts_cbranch_scc1 .Lse_b2l\n"
      ".Lse_b2x:\n"
      "\tv_add_f32 v24, v24, v28\n"
      "\tv_add_f32 v25, v26, v30\n"
      "\ts_nop 1\n"
      "\tv_permlane32_swap_b32 v24, v25\n"
      "\tv_add_f32 v24, v24, v25\n"
      "\ts_nop 1\n"
      "\tv_add_f32_dpp v24, v24, v24 row_ror:4 row_mask:0xf bank_mask:0xf\n"
      "\ts_nop 1\n"
      "\tv_add_f32_dpp v24, v24, v24 row_ror:8 row_mask:0xf bank_mask:0xf\n"
      "\tv_mov_b32 v26, v24\n"
      "\ts_nop 1\n"
      "\tv_permlane16_swap_b32 v24, v26\n"
      "\tv_add_f32 v24, v24, v26\n"
      "\ts_and_b64 exec, s[26:27], s[40:41]\n"
      "\tds_write_b32 v5, v24 offset:35712\n"
      "\ts_mov_b64 exec, s[26:27]\n"
      ".Lse_b2e:\n"
      "\ts_waitcnt lgkmcnt(0)\n"
      "\ts_cbranch_execz .Lse_ret\n"
      "\ts_barrier\n"
      "\ts_cmp_eq_u32 s24, 0\n"
      "\ts_cbranch_scc0 .Lse_b5\n"
      ".Lse_seg2:\n"
      "\tds_read_b64 v[24:25], v3 offset:35840\n"
      "\tv_mov_b32 v29, s28\n"
      "\ts_waitcnt lgkmcnt(0)\n"
      "\tv_add_f32_dpp v24, v24, v24 row_ror:4 row_mask:0xf bank_mask:0xf\n"
      "\tv_add_f32_dpp v25, v25, v25 row_ror:4 row_mask:0xf bank_mask:0xf\n"
      "\ts_nop 0\n"
      "\tv_add_f32_dpp v24, v24, v24 row_ror:8 row_mask:0xf bank_mask:0xf\n"
      "\tv_add_f32_dpp v25, v25, v25 row_ror:8 row_mask:0xf bank_mask:0xf\n"
      "\tv_mov_b32 v26, v24\n"
      "\tv_mov_b32 v27, v25\n"
      "\ts_nop 0\n"
      "\tv_permlane16_swap_b32 v24, v26\n"
      "\tv_permlane16_swap_b32 v25, v27\n"
      "\tv_pk_add_f32 v[24:25], v[24:25], v[26:27]\n"
      "\ts_nop 0\n"
      "\tv_pk_mul_f32 v[24:25], v[24:25], s[42:43]\n"
      "\ts_nop 0\n"
      "\tv_cvt_pk_bf16_f32 v28, v24, v25\n"
      "\ts_cbranch_execz .Lse_b4\n"
      "\tbuffer_store_dwordx2 v[28:29], v2, s[60:63], s38 offen sc1\n"
      "\tbuffer_store_dwordx2 v[28:29], v2, s[60:63], s52 offen sc1\n"
      ".Lse_b4:\n"
      "\ts_cmp_eq_u32 s24, 0\n"
      "\ts_cbranch_scc0 .Lse_b5\n"
      "\ts_and_b64 exec, s[26:27], s[44:45]\n"
      "\ts_mov_b32 s30, 0\n"
      "\ts_cbranch_execz .Lse_b4x\n"
      "\ts_mov_b32 s31, 0\n"
      ".Lse_tw2:\n"
      "\ts_memrealtime s[88:89]\n"
      "\ts_add_u32 s31, s31, 1\n"
      "\ts_waitcnt lgkmcnt(0)\n"
      "\ts_sub_u32 s88, s88, s6\n"
      "\ts_cmp_ge_i32 s88, 0\n"
      "\ts_cbranch_scc1 .Lse_tw2x\n"
      "\ts_cmp_lt_u32 s31, 0x400\n"
      "\ts_cbranch_scc1 .Lse_tw2\n"
      ".Lse_tw2x:\n"
      ".Lse_b4l:\n"
      "\tbuffer_load_dwordx4 v[24:27], v4, s[60:63], s39 offen sc1\n"
      "\ts_add_u32 s30, s30, 1\n"
      "\ts_waitcnt vmcnt(0)\n"
      "\tv_cmp_ne_u32 vcc, s28, v25\n"
      "\tv_cmp_ne_u32 s[46:47], s28, v27\n"
      "\ts_or_b64 s[46:47], vcc, s[46:47]\n"
      "\ts_cbranch_scc0 .Lse_b4x\n"
      "\ts_cmp_lt_u32 s30, 0x40000\n"
      "\ts_cbranch_scc1 .Lse_b4l\n"
      ".Lse_b4x:\n"
      "\tv_lshlrev_b32 v28, 16, v24\n"
      "\tv_and_b32 v29, s75, v24\n"
      "\tv_lshlrev_b32 v30, 16, v26\n"
      "\tv_and_b32 v31, s75, v26\n"
      "\tds_write_b128 v14, v[28:31] offset:36096\n"
      "\ts_mov_b64 exec, s[26:27]\n"
      ".Lse_b5:\n"
      "\ts_waitcnt lgkmcnt(0)\n"
      "\ts_cbranch_execz .Lse_ret\n"
      "\ts_barrier\n"
      ".Lse_seg3:\n"
      "\tds_read_b128 v[24:27], v14 offset:36096\n"
      "\ts_waitcnt lgkmcnt(0)\n"
      "\tv_pk_mul_f32 v[24:25], v[24:25], v[36:37]\n"
      "\ts_nop 0\n"
      "\tv_pk_fma_f32 v[24:25], v[26:27], v[38:39], v[24:25]\n"
      "\ts_nop 0\n"
      "\tv_add_f32 v24, v24, v25\n"
      "\ts_nop 1\n"
      "\tv_add_f32_dpp v24, v24, v24 quad_perm:[1,0,3,2] row_mask:0xf bank_mask:0xf\n"
      "\ts_nop 1\n"
      "\tv_add_f32_dpp v24, v24, v24 quad_perm:[2,3,0,1] row_mask:0xf bank_mask:0xf\n"
      "\ts_nop 1\n"
      "\tv_add_f32_dpp v24, v24, v24 row_ror:4 row_mask:0xf bank_mask:0xf\n"
      "\ts_nop 1\n"
      "\tv_add_f32_dpp v24, v24, v24 row_ror:8 row_mask:0xf bank_mask:0xf\n"
      "\ts_nop 1\n"
      "\tv_add_f32_dpp v24, v24, v24 row_bcast:15 row_mask:0xa bank_mask:0xf\n"
      "\ts_nop 1\n"
      "\tv_add_f32_dpp v24, v24, v24 row_bcast:31 row_mask:0xc bank_mask:0xf\n"
      "\tv_cvt_pk_bf16_f32 v24, v24, v24\n"
      "\tv_and_b32 v24, s75, v24\n"
      "\tv_mul_f32 v25, 0xbfb8aa3b, v24\n"
      "\tv_exp_f32 v25, v25\n"
      "\ts_nop 0\n"
      "\tv_add_f32 v25, 1.0, v25\n"
      "\tv_rcp_f32 v25, v25\n"
      "\ts_and_b64 exec, s[26:27], s[48:49]\n"
      "\tv_mul_f32 v24, v24, v25\n"
      "\tv_cvt_pk_bf16_f32 v24, v24, v24\n"
      "\tv_and_b32 v24, s75, v24\n"
      "\tds_write_b32 v7, v24 offset:37120\n"
      "\ts_mov_b64 exec, s[26:27]\n"
      "\ts_waitcnt lgkmcnt(0)\n"
      "\ts_cbranch_execz .Lse_ret\n"
      "\ts_barrier\n"
      "\ts_cmp_lt_u32 s24, 3\n"
      "\ts_cbranch_scc0 .Lse_b8\n"
      ".Lse_seg4:\n"
      "\tds_read_b128 v[24:27], v8 offset:37120\n"
      "\tds_read_b128 v[28:31], v8 offset:37136\n"
      "\tds_read_b128 v[2:5], v8 offset:37152\n"
      "\ts_waitcnt lgkmcnt(2)\n"
      "\tv_pk_mul_f32 v[24:25], v[24:25], v[16:17]\n"
      "\tv_pk_mul_f32 v[26:27], v[26:27], v[18:19]\n"
      "\ts_waitcnt lgkmcnt(1)\n"
      "\tv_pk_fma_f32 v[24:25], v[28:29], v[20:21], v[24:25]\n"
      "\tv_pk_fma_f32 v[26:27], v[30:31], v[22:23], v[26:27]\n"
      "\ts_waitcnt lgkmcnt(0)\n"
      "\tv_pk_fma_f32 v[24:25], v[2:3], v[32:33], v[24:25]\n"
      "\tv_pk_fma_f32 v[26:27], v[4:5], v[34:35], v[26:27]\n"
      "\ts_nop 0\n"
      "\tv_pk_add_f32 v[24:25], v[24:25], v[26:27]\n"
      "\ts_nop 0\n"
      "\tv_add_f32 v24, v24, v25\n"
      "\tv_cvt_pk_bf16_f32 v24, v24, v24\n"
      "\tv_and_b32 v24, s75, v24\n"
      "\tv_mul_f32 v24, 0xbfb8aa3b, v24\n"
      "\tv_exp_f32 v24, v24\n"
      "\ts_nop 0\n"
      "\tv_add_f32 v24, 1.0, v24\n"
      "\tv_rcp_f32 v24, v24\n"
      "\ts_nop 0\n"
      "\tv_cvt_pk_bf16_f32 v24, v24, v24\n"
      "\tv_and_b32 v24, s75, v24\n"
      "\tds_write_b32 v6, v24 offset:37184\n"
      ".Lse_b8:\n"
      "\ts_waitcnt lgkmcnt(0)\n"
      "\ts_cbranch_execz .Lse_ret\n"
      "\ts_barrier\n"
      ".Lse_seg5:\n"
      "\tds_read_b128 v[16:19], v13 offset:37184\n"
      "\tds_read_b128 v[20:23], v13 offset:37200\n"
      "\ts_memrealtime s[88:89]\n"
      "\ts_mov_b32 s85, 0\n"
      "\ts_cmp_eq_u32 s50, 0\n"
      "\ts_cbranch_scc0 .Lse_b9\n"
      "\tv_mov_b32 v24, s28\n"
      "\ts_mov_b64 exec, 1\n"
      "\tbuffer_store_dword v24, off, s[60:63], 0 sc1\n"
      "\ts_mov_b64 exec, -1\n"
      ".Lse_b9:\n"
      "\ts_waitcnt lgkmcnt(0)\n"
      "\ts_mov_b32 s87, s88\n"
      ".Lse_store:\n"
      "\ts_set_gpr_idx_on s85, gpr_idx(SRC1)\n"
      "\tv_lshlrev_b32 v24, 16, v40\n"
      "\tv_and_b32 v25, s75, v40\n"
      "\tv_lshlrev_b32 v26, 16, v41\n"
      "\tv_and_b32 v27, s75, v41\n"
      "\tv_lshlrev_b32 v28, 16, v42\n"
      "\tv_and_b32 v29, s75, v42\n"
      "\tv_lshlrev_b32 v30, 16, v43\n"
      "\tv_and_b32 v31, s75, v43\n"
      "\ts_set_gpr_idx_off\n"
      "\ts_mov_b32 s95, 0\n"
      "\tv_pk_mul_f32 v[24:25], v[24:25], v[16:17]\n"
      "\tv_pk_mul_f32 v[26:27], v[26:27], v[18:19]\n"
      "\tv_pk_mul_f32 v[28:29], v[28:29], v[20:21]\n"
      "\tv_pk_mul_f32 v[30:31], v[30:31], v[22:23]\n"
      "\tv_cvt_pk_bf16_f32 v32, v24, v25\n"
      "\tv_cvt_pk_bf16_f32 v33, v26, v27\n"
      "\tv_cvt_pk_bf16_f32 v34, v28, v29\n"
      "\tv_cvt_pk_bf16_f32 v35, v30, v31\n"
      ".Lse_sw:\n"
      "\ts_memrealtime s[88:89]\n"
      "\ts_add_u32 s95, s95, 1\n"
      "\ts_waitcnt lgkmcnt(0)\n"
      "\ts_sub_u32 s88, s88, s87\n"
      "\ts_lshl_b32 s88, s88, 4\n"
      "\ts_cmp_ge_i32 s88, s86\n"
      "\ts_cbranch_scc1 .Lse_go\n"
      "\ts_sleep 1\n"
      "\ts_cmp_lt_u32 s95, 0x1000\n"
      "\ts_cbranch_scc1 .Lse_sw\n"
      ".Lse_go:\n"
      "\ts_cbranch_execz .Lse_ret\n"
      "\tbuffer_store_dwordx4 v[32:35], v15, s[80:83], s84 offen sc1\n"
      "\ts_add_u32 s84, s84, s69\n"
      "\ts_addk_i32 s86, 614\n"
      "\ts_add_u32 s85, s85, 4\n"
      "\ts_cmpk_lt_u32 s85, 128\n"
      "\ts_cbranch_scc1 .Lse_store\n"
      "\ts_endpgm\n"
      ".Lse_ret:\n"
      "\ts_setpc_b64 s[92:93]\n"
      :
      : "{v0}"(threadIdx.x), [b] "s"(blockIdx.x), [ka] "s"((u64)__builtin_amdgcn_kernarg_segment_ptr()),
        "i"((LAS char*)smem)
      :
        "v1", "v2", "v3", "v4", "v5", "v6", "v7", "v8", "v9", "v10", "v11", "v12", "v13", "v14", "v15", "v16",
        "v17", "v18", "v19", "v20", "v21", "v22", "v23", "v24", "v25", "v26", "v27", "v28", "v29", "v30", "v31",
        "v32", "v33", "v34", "v35", "v36", "v37", "v38", "v39", "v40", "v41", "v42", "v43", "v44", "v45", "v46",
        "v47", "v48", "v49", "v50", "v51", "v52", "v53", "v54", "v55", "v56", "v57", "v58", "v59", "v60", "v61",
        "v62", "v63", "v64", "v65", "v66", "v67", "v68", "v69", "v70", "v71", "v72", "v73", "v74", "v75", "v76",
        "v77", "v78", "v79", "v80", "v81", "v82", "v83", "v84", "v85", "v86", "v87", "v88", "v89", "v90", "v91",
        "v92", "v93", "v94", "v95", "v96", "v97", "v98", "v99", "v100", "v101", "v102", "v103", "v104", "v105",
        "v106", "v107", "v108", "v109", "v110", "v111", "v112", "v113", "v114", "v115", "v116", "v117", "v118",
        "v119", "v120", "v121", "v122", "v123", "v124", "v125", "v126", "v127", "v128", "v129", "v130", "v131",
        "v132", "v133", "v134", "v135", "v136", "v137", "v138", "v139", "v140", "v141", "v142", "v143", "v144",
        "v145", "v146", "v147", "v148", "v149", "v150", "v151", "v152", "v153", "v154", "v155", "v156", "v157",
        "v158", "v159", "v160", "v161", "v162", "v163", "v164", "v165", "v166", "v167", "s5", "s6", "s7", "s8",
        "s9", "s10", "s11", "s12", "s13", "s14", "s15", "s16", "s17", "s18", "s19", "s20", "s21", "s22", "s23",
        "s24", "s25", "s26", "s27", "s28", "s29", "s30", "s31", "s34", "s35", "s36", "s37", "s38", "s39", "s40",
        "s41", "s42", "s43", "s44", "s45", "s46", "s47", "s48", "s49", "s50", "s51", "s52", "s53", "s54", "s55",
        "s56", "s57", "s58", "s59", "s60", "s61", "s62", "s63", "s64", "s65", "s66", "s67", "s68", "s69", "s70",
        "s71", "s72", "s73", "s74", "s75", "s76", "s77", "s78", "s79", "s80", "s81", "s82", "s83", "s84", "s85",
        "s86", "s87", "s88", "s89", "s90", "s91", "s92", "s93", "s94", "s95", "s96", "s97", "s98", "s99", "vcc",
        "scc", "memory");
}

extern "C" void kernel_launch(void* const* d_in, const int* in_sizes, int n_in, void* d_out, int out_size,
                              void* d_ws, size_t ws_size, hipStream_t stream) {
  se_fused<<<256, 768, 0, stream>>>(d_in[0], d_in[1], d_in[2], d_out, d_ws);
}
```
